```python
import torch

def _softmax(x, axis=-1):
    return torch.softmax(x, dim=axis)
def _gelu(x):
    return torch.nn.functional.gelu(x, approximate="tanh")
def _silu(x):
    return torch.nn.functional.silu(x)
def _sigmoid(x):
    return torch.sigmoid(x)
def _layernorm(x, axis=-1, eps=1e-5):
    m = x.mean(dim=axis, keepdim=True)
    v = x.var(dim=axis, keepdim=True, unbiased=False)
    return (x - m) / torch.sqrt(v + eps)
def _scatter_add(tgt, idx, src, axis=0):
    return tgt.clone().index_add_(axis, idx, src)
def _segment_sum(src, seg, n):
    out = torch.zeros((n,) + src.shape[1:], dtype=src.dtype)
    return out.index_add_(0, seg, src)
def _pad(x, pw):
    flat = tuple(v for pair in reversed(pw) for v in pair)
    return torch.nn.functional.pad(x, flat)

kernel_name = 'gated_mix__sliding_block_cached__glu_gated_swiglu__decode'

def setup_inputs():

    _w = (lambda a: (lambda t: t.to(torch.bfloat16) if t.is_floating_point() else t)(torch.as_tensor(a)))
    def _A_setup():
        B, Sq, D = 8, 1, 640
        Hq, Hkv, Sk, Dh = 32, 32, 4096, 128
        assert Hq % Hkv == 0, (Hq, Hkv)
        return {
            "x": _w((lambda *s: torch.randn(*s, dtype=torch.bfloat16) * 0.05)(B, Sq, D)),
            "k_cache": _w((lambda *s: torch.randn(*s, dtype=torch.bfloat16) * 6.4)(B, Hkv, Sk, Dh)),
            "v_cache": _w((lambda *s: torch.randn(*s, dtype=torch.bfloat16) * 0.05)(B, Hkv, Sk, Dh)),
            "wq": _w((lambda *s: torch.randn(*s, dtype=torch.bfloat16) * 6.4)(D, Hq * Dh) * (D ** -0.5)),
            "wo": _w((lambda *s: torch.randn(*s, dtype=torch.bfloat16) * 0.05)(Hq * Dh, D) * ((Hq * Dh) ** -0.5)),
        }
    _A_in = _A_setup()

    def _B_setup():
        B, S, D, F = 8, 1, 640, 1728
        return {
            "x": _w((lambda *s: torch.randn(*s, dtype=torch.bfloat16) * 0.05)(B, S, D)),
            "w1": _w((lambda *s: torch.randn(*s, dtype=torch.bfloat16) * 0.4)(D, F) * (2.0 / D) ** 0.5),
            "w3": _w((lambda *s: torch.randn(*s, dtype=torch.bfloat16) * 0.4)(D, F) * (2.0 / D) ** 0.5),
            "w2": _w((lambda *s: torch.randn(*s, dtype=torch.bfloat16) * 0.4)(F, D) * (2.0 / F) ** 0.5),
        }
    _B_in = _B_setup()

    return {
        "A_x": _A_in["x"],
        "A_k_cache": _A_in["k_cache"],
        "A_v_cache": _A_in["v_cache"],
        "A_wq": _A_in["wq"],
        "A_wo": _A_in["wo"],
        "B_x": _B_in["x"],
        "B_w1": _B_in["w1"],
        "B_w3": _B_in["w3"],
        "B_w2": _B_in["w2"],
        "gate_w": _w((lambda *s: torch.randn(*s, dtype=torch.bfloat16) * 0.05)(640) * 16.0),
    }

def reference(A_x, A_k_cache, A_v_cache, A_wq, A_wo, B_x, B_w1, B_w3, B_w2, gate_w):
    def _A_ref(x, k_cache, v_cache, wq, wo):
        B, Sq, D = x.shape
        Hq, Dh = 32, 128
        Hkv, Sk = k_cache.shape[1], k_cache.shape[2]
        W = min(4096, max(256, Sk // 4))
        rep = Hq // Hkv
        q = (lambda x, *d: x.permute(*d))((x @ wq).reshape(B, Sq, Hq, Dh), 0, 2, 1, 3)
        k = (lambda x, n, axis: torch.repeat_interleave(x, n, dim=axis))(k_cache[:, :, Sk - W:, :], rep, axis=1)
        v = (lambda x, n, axis: torch.repeat_interleave(x, n, dim=axis))(v_cache[:, :, Sk - W:, :], rep, axis=1)
        scores = torch.einsum("bhqd,bhkd->bhqk", q, k) * (Dh ** -0.5)
        p = _softmax(scores, axis=-1)
        o = torch.einsum("bhqk,bhkd->bhqd", p, v)
        return (lambda x, *d: x.permute(*d))(o, 0, 2, 1, 3).reshape(B, Sq, Hq * Dh) @ wo

    def _B_ref(x, w1, w3, w2):
        gate = torch.einsum("bsd,df->bsf", x, w1)
        up = torch.einsum("bsd,df->bsf", x, w3)
        h = _silu(gate) * up
        out = torch.einsum("bsf,fd->bsd", h, w2)
        return out

    x = A_x
    g = _sigmoid((x * gate_w).sum(-1, keepdims=True))
    a = _A_ref(x, A_k_cache, A_v_cache, A_wq, A_wo)
    b = _B_ref(x, B_w1, B_w3, B_w2)
    return g * a + (1 - g) * b
```

```cpp
#include <hip/hip_runtime.h>
#include <hip/hip_bf16.h>
#include <stdint.h>

#define RING 18
#define KV_AUX 2
#define W_AUX 0

typedef unsigned u32x4 __attribute__((ext_vector_type(4)));
typedef float f32x4 __attribute__((ext_vector_type(4)));
typedef float f32x2 __attribute__((ext_vector_type(2)));
typedef __bf16 bf16x2 __attribute__((ext_vector_type(2)));
typedef __attribute__((address_space(1))) unsigned gu32;
typedef __attribute__((address_space(1))) unsigned long long gu64;
#define RLX_AGENT __ATOMIC_RELAXED, __HIP_MEMORY_SCOPE_AGENT

constexpr int NB = 8, DM = 640, NH = 32, SK = 4096, DH = 128, WIN = 1024, FF = 1728;
constexpr int NFG = FF / 64;
constexpr int RSRC_W3 = 0x00020000;
constexpr size_t WS_EPOCH = 0;
constexpr size_t WS_INBOX = 4096;

__device__ __forceinline__ float dot2(unsigned a, unsigned b, float c) {
  return __builtin_amdgcn_fdot2_f32_bf16(__builtin_bit_cast(bf16x2, a), __builtin_bit_cast(bf16x2, b), c, false);
}
__device__ __forceinline__ unsigned pk_bf16(float lo, float hi) {
  f32x2 v = {lo, hi};
  return __builtin_bit_cast(unsigned, __builtin_convertvector(v, bf16x2));
}
__device__ __forceinline__ float rbf(float x) { return __uint_as_float(pk_bf16(x, x) << 16); }
__device__ __forceinline__ unsigned lo_pair(unsigned a, unsigned b) { return __builtin_amdgcn_perm(b, a, 0x05040100u); }
__device__ __forceinline__ unsigned hi_pair(unsigned a, unsigned b) { return __builtin_amdgcn_perm(b, a, 0x07060302u); }
template <int CTRL> __device__ __forceinline__ float dpp(float x) {
  return __builtin_bit_cast(float, __builtin_amdgcn_mov_dpp(__builtin_bit_cast(int, x), CTRL, 0xf, 0xf, true));
}
template <int CTRL> __device__ __forceinline__ unsigned dppu(unsigned x) {
  return (unsigned)__builtin_amdgcn_mov_dpp((int)x, CTRL, 0xf, 0xf, true);
}
constexpr int XOR1 = 0xB1, XOR2 = 0x4E, XOR7 = 0x141, XOR8 = 0x128, BCAST = 0x150;
__device__ __forceinline__ float swap32_add(float a, float b) {
  auto t = __builtin_amdgcn_permlane32_swap(__float_as_uint(a), __float_as_uint(b), false, false);
  return __uint_as_float(t[0]) + __uint_as_float(t[1]);
}
__device__ __forceinline__ float swap16_add(float a, float b) {
  auto t = __builtin_amdgcn_permlane16_swap(__float_as_uint(a), __float_as_uint(b), false, false);
  return __uint_as_float(t[0]) + __uint_as_float(t[1]);
}
__device__ __forceinline__ float wave_max(float x) {
  x = fmaxf(x, dpp<XOR1>(x)); x = fmaxf(x, dpp<XOR2>(x)); x = fmaxf(x, dpp<XOR7>(x)); x = fmaxf(x, dpp<XOR8>(x));
  auto s = __builtin_amdgcn_permlane16_swap(__float_as_uint(x), __float_as_uint(x), false, false);
  x = fmaxf(__uint_as_float(s[0]), __uint_as_float(s[1]));
  auto t = __builtin_amdgcn_permlane32_swap(__float_as_uint(x), __float_as_uint(x), false, false);
  return fmaxf(__uint_as_float(t[0]), __uint_as_float(t[1]));
}
__device__ __forceinline__ float wave_sum(float x) {
  x += dpp<XOR1>(x); x += dpp<XOR2>(x); x += dpp<XOR7>(x); x += dpp<XOR8>(x);
  x = swap16_add(x, x);
  return swap32_add(x, x);
}
__device__ __forceinline__ float transpose_reduce16(float (&v)[16], int c) {
  const bool b3 = c & 8, b2 = c & 4, b1 = c & 2, b0 = c & 1;
#pragma unroll
  for (int i = 0; i < 8; ++i) { float k = b3 ? v[i + 8] : v[i], s = b3 ? v[i] : v[i + 8]; v[i] = k + dpp<XOR8>(s); }
#pragma unroll
  for (int i = 0; i < 4; ++i) { float k = b2 ? v[i + 4] : v[i], s = b2 ? v[i] : v[i + 4]; v[i] = k + dpp<XOR7>(s); }
#pragma unroll
  for (int i = 0; i < 2; ++i) { float k = b1 ? v[i + 2] : v[i], s = b1 ? v[i] : v[i + 2]; v[i] = k + dpp<XOR2>(s); }
  { float k = b0 ? v[1] : v[0], s = b0 ? v[0] : v[1]; v[0] = k + dpp<XOR1>(s); }
  return v[0];
}
__device__ __forceinline__ f32x2 rows_reduce8(const float (&a)[8]) {
  float n0 = swap32_add(a[0], a[4]), n1 = swap32_add(a[1], a[5]), n2 = swap32_add(a[2], a[6]), n3 = swap32_add(a[3], a[7]);
  f32x2 r = {swap16_add(n0, n2), swap16_add(n1, n3)};
  return r;
}
__device__ __forceinline__ void wg_barrier() {
  asm volatile("s_waitcnt lgkmcnt(0)\n\ts_barrier" ::: "memory");
}
template <int N> struct IC { static constexpr int v = N; };
template <int I, int E, class F> __device__ __forceinline__ void sfor(F&& f) { if constexpr (I < E) { f(IC<I>{}); sfor<I + 1, E>(f); } }

struct Lds {
  float qpart[8][128];
  float gpart[8][128];
  float opart[8][128];
  float ml[8][2];
  float part[8][DM];
  float fin[NH][20];
  unsigned cnt, tag;
};

__global__ void __launch_bounds__(1024, 4)
gated_mix_kernel(const unsigned* __restrict__ x32, const void* __restrict__ kc, const void* __restrict__ vc,
                 const void* __restrict__ wq, const void* __restrict__ wo, const void* __restrict__ w1,
                 const void* __restrict__ w3, const void* __restrict__ w2, const unsigned* __restrict__ gw32,
                 __hip_bfloat16* __restrict__ out, unsigned char* __restrict__ ws) {
  __shared__ Lds L;
  const int tid = threadIdx.x, lane = tid & 63;
  const int wave = __builtin_amdgcn_readfirstlane(tid >> 6);
  const int bid = blockIdx.x;
  const int b = (bid >> 3) & 7, h = (bid & 7) + 8 * (bid >> 6);
  const int r = lane >> 4, c = lane & 15;
  gu32* epoch = (gu32*)(ws + WS_EPOCH + 256 * b);
  gu64* inbox = (gu64*)(ws + WS_INBOX);
  float outacc[10];
  unsigned tag;

  if (wave < 8) {
    const size_t slab = ((size_t)(b * NH + h) * SK + (SK - WIN) + wave * 128) * DH * 2;
    const __amdgpu_buffer_rsrc_t rk = __builtin_amdgcn_make_buffer_rsrc((char*)kc + slab, 0, 128 * DH * 2, RSRC_W3);
    const __amdgpu_buffer_rsrc_t rv = __builtin_amdgcn_make_buffer_rsrc((char*)vc + slab, 0, 128 * DH * 2, RSRC_W3);
    const int voff = lane * 16;
    u32x4 ring[RING];
    auto issue = [&](auto J) {
      constexpr int j = J.v;
      if constexpr (j < 32) ring[j % RING] = __builtin_amdgcn_raw_buffer_load_b128(rk, voff + (j & 3) * 1024, (j >> 2) * 4096, KV_AUX);
      else if constexpr (j < 64) {
        constexpr int i = ((j - 32) >> 1) + 16 * (j & 1);
        ring[j % RING] = __builtin_amdgcn_raw_buffer_load_b128(rv, voff + (i & 3) * 1024, (i >> 2) * 4096, KV_AUX);
      }
    };
    sfor<0, RING>(issue);
    __builtin_amdgcn_sched_barrier(0);
    wg_barrier();
    unsigned q[4];
    {
      f32x4 s0 = {0, 0, 0, 0}, s1 = {0, 0, 0, 0};
#pragma unroll
      for (int w = 0; w < 8; ++w) { s0 += *(const f32x4*)&L.qpart[w][8 * c]; s1 += *(const f32x4*)&L.qpart[w][8 * c + 4]; }
      q[0] = pk_bf16(s0[0], s0[1]); q[1] = pk_bf16(s0[2], s0[3]); q[2] = pk_bf16(s1[0], s1[1]); q[3] = pk_bf16(s1[2], s1[3]);
    }
    float sc[2], sp[16];
    sfor<0, 32>([&](auto J) {
      constexpr int j = J.v;
      const u32x4 k = ring[j % RING];
      sp[j & 15] = dot2(k[3], q[3], dot2(k[2], q[2], dot2(k[1], q[1], dot2(k[0], q[0], 0.f))));
      __builtin_amdgcn_sched_barrier(0);
      issue(IC<j + RING>{});
      __builtin_amdgcn_sched_barrier(0);
      if constexpr ((j & 15) == 15) sc[j >> 4] = transpose_reduce16(sp, c);
    });
    const float s0 = rbf(rbf(sc[0]) * 0.08838834764831845f), s1 = rbf(rbf(sc[1]) * 0.08838834764831845f);
    const float m = wave_max(fmaxf(s0, s1));
    const float p0 = __expf(s0 - m), p1 = __expf(s1 - m);
    const float l = wave_sum(p0 + p1);
    const unsigned pp = pk_bf16(p0, p1);
    float acc[8] = {0, 0, 0, 0, 0, 0, 0, 0};
    sfor<0, 16>([&](auto I) {
      constexpr int i = I.v, j = 32 + 2 * i;
      const u32x4 va = ring[j % RING], vb = ring[(j + 1) % RING];
      const unsigned pb = dppu<BCAST + i>(pp);
#pragma unroll
      for (int e = 0; e < 4; ++e) {
        acc[2 * e] = dot2(lo_pair(va[e], vb[e]), pb, acc[2 * e]);
        acc[2 * e + 1] = dot2(hi_pair(va[e], vb[e]), pb, acc[2 * e + 1]);
      }
      __builtin_amdgcn_sched_barrier(0);
      issue(IC<j + RING>{}); issue(IC<j + 1 + RING>{});
      __builtin_amdgcn_sched_barrier(0);
    });
    const f32x2 o2 = rows_reduce8(acc);
    *(f32x2*)&L.opart[wave][8 * c + 4 * (r >> 1) + 2 * (r & 1)] = o2;
    if (lane == 0) { L.ml[wave][0] = m; L.ml[wave][1] = l; }
    tag = L.tag;
    wg_barrier();
  } else {
    const int hw = wave - 8, rg = hw * 4 + r;
    unsigned xs[10];
#pragma unroll
    for (int k = 0; k < 10; ++k) xs[k] = x32[b * (DM / 2) + rg + 32 * k];
    tag = __hip_atomic_load(epoch, RLX_AGENT) + 1u;
    u32x4 wb[20];
    {
      const __amdgpu_buffer_rsrc_t rq = __builtin_amdgcn_make_buffer_rsrc((char*)wq + h * 256, 0, DM * NH * DH * 2 - h * 256, RSRC_W3);
      const int voff = 2 * rg * 8192 + c * 16;
#pragma unroll
      for (int k = 0; k < 20; ++k) wb[k] = __builtin_amdgcn_raw_buffer_load_b128(rq, voff, (64 * (k >> 1) + (k & 1)) * 8192, W_AUX);
    }
    unsigned xg[5], gg[5];
#pragma unroll
    for (int k = 0; k < 5; ++k) { xg[k] = x32[b * (DM / 2) + lane + 64 * k]; gg[k] = gw32[lane + 64 * k]; }
    __builtin_amdgcn_sched_barrier(0);
    {
      const int t = tid - 512;
      __hip_atomic_store(inbox + ((size_t)((b * NH + t / 20) * NH + h) * 20 + t % 20), (unsigned long long)(tag - 1u) << 32, RLX_AGENT);
      if (t < 128) { const int u = t + 512;
        __hip_atomic_store(inbox + ((size_t)((b * NH + u / 20) * NH + h) * 20 + u % 20), (unsigned long long)(tag - 1u) << 32, RLX_AGENT); }
    }
    auto gemv_rows = [&](float (*dst)[128]) {
      float a[8] = {0, 0, 0, 0, 0, 0, 0, 0};
#pragma unroll
      for (int k = 0; k < 10; ++k)
#pragma unroll
        for (int e = 0; e < 4; ++e) {
          a[2 * e] = dot2(lo_pair(wb[2 * k][e], wb[2 * k + 1][e]), xs[k], a[2 * e]);
          a[2 * e + 1] = dot2(hi_pair(wb[2 * k][e], wb[2 * k + 1][e]), xs[k], a[2 * e + 1]);
        }
      *(f32x2*)&dst[hw][8 * c + 4 * (r >> 1) + 2 * (r & 1)] = rows_reduce8(a);
    };
    gemv_rows(L.qpart);
    if (tid == 512) { L.cnt = 0; L.tag = tag; }
    __builtin_amdgcn_sched_barrier(0);
    wg_barrier();
    float g;
    {
      float s = 0.f;
#pragma unroll
      for (int k = 0; k < 5; ++k) {
        s += rbf(__uint_as_float(xg[k] << 16) * __uint_as_float(gg[k] << 16));
        s += rbf(__uint_as_float(xg[k] & 0xffff0000u) * __uint_as_float(gg[k] & 0xffff0000u));
      }
      s = rbf(wave_sum(s));
      g = rbf(1.f / (1.f + __expf(-s)));
    }
    const float omg = rbf(1.f - g);
#pragma unroll
    for (int e = 0; e < 10; ++e) outacc[e] = 0.f;
    if (h < NFG) {
      const char* wp = (const char*)(c < 8 ? w1 : w3) + (size_t)(2 * rg) * (FF * 2) + h * 128 + (c & 7) * 16;
#pragma unroll
      for (int k = 0; k < 20; ++k) wb[k] = *(const u32x4*)(wp + (64 * (k >> 1) + (k & 1)) * (FF * 2));
      __builtin_amdgcn_sched_barrier(0);
      gemv_rows(L.gpart);
      __builtin_amdgcn_sched_barrier(0);
      const __amdgpu_buffer_rsrc_t r2 = __builtin_amdgcn_make_buffer_rsrc((char*)w2 + (size_t)(h * 64 + hw * 8) * 1280, 0, 8 * 1280, RSRC_W3);
      u32x4 da[8]; unsigned db[8];
#pragma unroll
      for (int k = 0; k < 8; ++k) {
        da[k] = __builtin_amdgcn_raw_buffer_load_b128(r2, lane * 16, k * 1280, W_AUX);
        db[k] = __builtin_amdgcn_raw_buffer_load_b32(r2, lane * 4 + 1024, k * 1280, W_AUX);
      }
      __builtin_amdgcn_sched_barrier(0);
      asm volatile("s_waitcnt lgkmcnt(0)" ::: "memory");
      if (lane == 0) __hip_atomic_fetch_add(&L.cnt, 1u, __ATOMIC_RELAXED, __HIP_MEMORY_SCOPE_WORKGROUP);
      while (__hip_atomic_load(&L.cnt, __ATOMIC_RELAXED, __HIP_MEMORY_SCOPE_WORKGROUP) < 8u) __builtin_amdgcn_s_sleep(2);
      asm volatile("" ::: "memory");
      float ga = 0.f, up = 0.f;
      const int f = hw * 8 + (lane & 7);
#pragma unroll
      for (int w = 0; w < 8; ++w) { ga += L.gpart[w][f]; up += L.gpart[w][64 + f]; }
      ga = rbf(ga); up = rbf(up);
      const float hv = rbf(ga / (1.f + __expf(-ga))) * up;
      const unsigned hp = pk_bf16(hv, dpp<XOR1>(hv));
#pragma unroll
      for (int k = 0; k < 4; ++k) {
        const unsigned hk = __builtin_amdgcn_readlane(hp, 2 * k);
#pragma unroll
        for (int e = 0; e < 4; ++e) {
          outacc[2 * e] = dot2(lo_pair(da[2 * k][e], da[2 * k + 1][e]), hk, outacc[2 * e]);
          outacc[2 * e + 1] = dot2(hi_pair(da[2 * k][e], da[2 * k + 1][e]), hk, outacc[2 * e + 1]);
        }
        outacc[8] = dot2(lo_pair(db[2 * k], db[2 * k + 1]), hk, outacc[8]);
        outacc[9] = dot2(hi_pair(db[2 * k], db[2 * k + 1]), hk, outacc[9]);
      }
    }
    __builtin_amdgcn_sched_barrier(0);
    const __amdgpu_buffer_rsrc_t ro = __builtin_amdgcn_make_buffer_rsrc((char*)wo + (size_t)(h * 128 + hw * 16) * 1280, 0, 16 * 1280, RSRC_W3);
    u32x4 oa[16]; unsigned ob[16];
#pragma unroll
    for (int k = 0; k < 16; ++k) {
      oa[k] = __builtin_amdgcn_raw_buffer_load_b128(ro, lane * 16, k * 1280, W_AUX);
      ob[k] = __builtin_amdgcn_raw_buffer_load_b32(ro, lane * 4 + 1024, k * 1280, W_AUX);
    }
#pragma unroll
    for (int k = 0; k < 8; ++k) {
#pragma unroll
      for (int e = 0; e < 4; ++e) {
        unsigned lo = lo_pair(oa[2 * k][e], oa[2 * k + 1][e]), hi = hi_pair(oa[2 * k][e], oa[2 * k + 1][e]);
        asm volatile("" : "+v"(lo), "+v"(hi));
        oa[2 * k][e] = lo; oa[2 * k + 1][e] = hi;
      }
      unsigned lo = lo_pair(ob[2 * k], ob[2 * k + 1]), hi = hi_pair(ob[2 * k], ob[2 * k + 1]);
      asm volatile("" : "+v"(lo), "+v"(hi));
      ob[2 * k] = lo; ob[2 * k + 1] = hi;
    }
    asm volatile("s_waitcnt vmcnt(0)" ::: "memory");
    __builtin_amdgcn_sched_barrier(0);
    wg_barrier();
    float M = L.ml[0][0];
#pragma unroll
    for (int w = 1; w < 8; ++w) M = fmaxf(M, L.ml[w][0]);
    float lsum = 0.f, ov = 0.f;
#pragma unroll
    for (int w = 0; w < 8; ++w) { const float fw = __expf(L.ml[w][0] - M); lsum += fw * L.ml[w][1]; ov += fw * L.opart[w][hw * 16 + c]; }
    ov = ov / lsum;
    const unsigned op = pk_bf16(ov, dpp<XOR1>(ov));
    float a[10] = {0, 0, 0, 0, 0, 0, 0, 0, 0, 0};
#pragma unroll
    for (int k = 0; k < 8; ++k) {
      const unsigned ok = __builtin_amdgcn_readlane(op, 2 * k);
#pragma unroll
      for (int e = 0; e < 4; ++e) { a[2 * e] = dot2(oa[2 * k][e], ok, a[2 * e]); a[2 * e + 1] = dot2(oa[2 * k + 1][e], ok, a[2 * e + 1]); }
      a[8] = dot2(ob[2 * k], ok, a[8]); a[9] = dot2(ob[2 * k + 1], ok, a[9]);
    }
#pragma unroll
    for (int e = 0; e < 10; ++e) a[e] = g * a[e] + omg * outacc[e];
    *(f32x4*)&L.part[hw][lane * 8] = f32x4{a[0], a[1], a[2], a[3]};
    *(f32x4*)&L.part[hw][lane * 8 + 4] = f32x4{a[4], a[5], a[6], a[7]};
    *(f32x2*)&L.part[hw][512 + lane * 2] = f32x2{a[8], a[9]};
  }
  wg_barrier();
  if (tid < DM) {
    float s = 0.f;
#pragma unroll
    for (int w = 0; w < 8; ++w) s += L.part[w][tid];
    __hip_atomic_store(inbox + ((size_t)((b * NH + tid / 20) * NH + h) * 20 + tid % 20),
                       ((unsigned long long)tag << 32) | __float_as_uint(s), RLX_AGENT);
    gu64* mine = inbox + (size_t)(b * NH + h) * (NH * 20) + tid;
    unsigned long long v;
    for (unsigned spins = 0; spins < (1u << 22); ++spins) {
      v = __hip_atomic_load(mine, RLX_AGENT);
      if (__all((unsigned)(v >> 32) == tag)) break;
      __builtin_amdgcn_s_sleep(1);
    }
    (&L.fin[0][0])[tid] = __uint_as_float((unsigned)v);
  }
  wg_barrier();
  if (tid < 20) {
    float s = 0.f;
#pragma unroll
    for (int p = 0; p < NH; ++p) s += L.fin[p][tid];
    out[b * DM + h * 20 + tid] = __float2bfloat16(s);
    if (tid == 0 && h == 0) __hip_atomic_store(epoch, tag, RLX_AGENT);
  }
}

extern "C" void kernel_launch(void* const* d_in, const int* in_sizes, int n_in, void* d_out, int out_size,
                              void* d_ws, size_t ws_size, hipStream_t stream) {
  gated_mix_kernel<<<NB * NH, 1024, 0, stream>>>((const unsigned*)d_in[0], d_in[1], d_in[2], d_in[3], d_in[4], d_in[6],
                                                 d_in[7], d_in[8], (const unsigned*)d_in[9], (__hip_bfloat16*)d_out,
                                                 (unsigned char*)d_ws);
}
```

```cpp
#include <hip/hip_runtime.h>
#include <hip/hip_bf16.h>
#include <stdint.h>

#define PRE 28
#define FLY 4
#define EXP 0
#define KV_AUX 2
#define W_AUX 2

typedef unsigned u32x4 __attribute__((ext_vector_type(4)));
typedef float f32x4 __attribute__((ext_vector_type(4)));
typedef float f32x2 __attribute__((ext_vector_type(2)));
typedef __bf16 bf16x2 __attribute__((ext_vector_type(2)));
typedef __attribute__((address_space(1))) unsigned gu32;
typedef __attribute__((address_space(1))) unsigned long long gu64;
#define RLX_AGENT __ATOMIC_RELAXED, __HIP_MEMORY_SCOPE_AGENT

constexpr int NB = 8, DM = 640, NH = 32, SK = 4096, DH = 128, WIN = 1024, FF = 1728;
constexpr int NFG = FF / 64;
constexpr int RSRC_W3 = 0x00020000;
constexpr int SC1 = 16;
constexpr unsigned WS_FLAG = 4096;
constexpr unsigned WS_X1 = 8192;
constexpr unsigned WS_X2 = WS_X1 + NH * 6144;
constexpr unsigned WS_X0 = WS_X2 + 8 * 20 * 8192;
constexpr unsigned WS_END = WS_X0 + NH * 65536;
constexpr unsigned SPIN_MAX = 1u << 20;
typedef unsigned u32x2 __attribute__((ext_vector_type(2)));
typedef __bf16 bf16x8 __attribute__((ext_vector_type(8)));
typedef short s16x4 __attribute__((ext_vector_type(4)));

__device__ __forceinline__ float dot2(unsigned a, unsigned b, float c) {
  return __builtin_amdgcn_fdot2_f32_bf16(__builtin_bit_cast(bf16x2, a), __builtin_bit_cast(bf16x2, b), c, false);
}
__device__ __forceinline__ unsigned pk_bf16(float lo, float hi) {
  f32x2 v = {lo, hi};
  return __builtin_bit_cast(unsigned, __builtin_convertvector(v, bf16x2));
}
__device__ __forceinline__ float rbf(float x) { return __uint_as_float(pk_bf16(x, x) << 16); }
__device__ __forceinline__ unsigned lo_pair(unsigned a, unsigned b) { return __builtin_amdgcn_perm(b, a, 0x05040100u); }
__device__ __forceinline__ unsigned hi_pair(unsigned a, unsigned b) { return __builtin_amdgcn_perm(b, a, 0x07060302u); }
template <int CTRL> __device__ __forceinline__ float dpp(float x) {
  return __builtin_bit_cast(float, __builtin_amdgcn_mov_dpp(__builtin_bit_cast(int, x), CTRL, 0xf, 0xf, true));
}
template <int CTRL> __device__ __forceinline__ unsigned dppu(unsigned x) {
  return (unsigned)__builtin_amdgcn_mov_dpp((int)x, CTRL, 0xf, 0xf, true);
}
constexpr int XOR1 = 0xB1, XOR2 = 0x4E, XOR7 = 0x141, XOR8 = 0x128, BCAST = 0x150;
__device__ __forceinline__ float swap32_add(float a, float b) {
  auto t = __builtin_amdgcn_permlane32_swap(__float_as_uint(a), __float_as_uint(b), false, false);
  return __uint_as_float(t[0]) + __uint_as_float(t[1]);
}
__device__ __forceinline__ float swap16_add(float a, float b) {
  auto t = __builtin_amdgcn_permlane16_swap(__float_as_uint(a), __float_as_uint(b), false, false);
  return __uint_as_float(t[0]) + __uint_as_float(t[1]);
}
__device__ __forceinline__ float wave_max(float x) {
  x = fmaxf(x, dpp<XOR1>(x)); x = fmaxf(x, dpp<XOR2>(x)); x = fmaxf(x, dpp<XOR7>(x)); x = fmaxf(x, dpp<XOR8>(x));
  auto s = __builtin_amdgcn_permlane16_swap(__float_as_uint(x), __float_as_uint(x), false, false);
  x = fmaxf(__uint_as_float(s[0]), __uint_as_float(s[1]));
  auto t = __builtin_amdgcn_permlane32_swap(__float_as_uint(x), __float_as_uint(x), false, false);
  return fmaxf(__uint_as_float(t[0]), __uint_as_float(t[1]));
}
__device__ __forceinline__ float wave_sum(float x) {
  x += dpp<XOR1>(x); x += dpp<XOR2>(x); x += dpp<XOR7>(x); x += dpp<XOR8>(x);
  x = swap16_add(x, x);
  return swap32_add(x, x);
}
template <int CTRL> __device__ __forceinline__ float fold(float lo, float hi, bool bit) {
  return (bit ? hi : lo) + dpp<CTRL>(bit ? lo : hi);
}
__device__ __forceinline__ f32x4 mfma32(u32x4 a, u32x4 b, f32x4 c) {
  return __builtin_amdgcn_mfma_f32_16x16x32_bf16(__builtin_bit_cast(bf16x8, a), __builtin_bit_cast(bf16x8, b), c, 0, 0, 0);
}
__device__ __forceinline__ f32x4 mfma16(u32x2 a, u32x2 b, f32x4 c) {
  return __builtin_amdgcn_mfma_f32_16x16x16bf16_1k(__builtin_bit_cast(s16x4, a), __builtin_bit_cast(s16x4, b), c, 0, 0, 0);
}
template <int N> __device__ __forceinline__ void wait_vm() { asm volatile("s_waitcnt vmcnt(%0)" ::"n"(N) : "memory"); }
__device__ __forceinline__ f32x2 rows_reduce8(const float (&a)[8]) {
  float n0 = swap32_add(a[0], a[4]), n1 = swap32_add(a[1], a[5]), n2 = swap32_add(a[2], a[6]), n3 = swap32_add(a[3], a[7]);
  f32x2 r = {swap16_add(n0, n2), swap16_add(n1, n3)};
  return r;
}
__device__ __forceinline__ void wg_barrier() {
  asm volatile("s_waitcnt lgkmcnt(0)\n\ts_barrier" ::: "memory");
}
template <int N> struct IC { static constexpr int v = N; };
template <int I, int E, class F> __device__ __forceinline__ void sfor(F&& f) { if constexpr (I < E) { f(IC<I>{}); sfor<I + 1, E>(f); } }

#define ST(i)
#define ST_FLUSH()

constexpr int WPITCH = 88;
struct Lds {
  unsigned short wblk[192][WPITCH];
  float opart[8][128];
  float ml[8][2];
  unsigned q[64];
  float g[8][2];
  unsigned cnt, tag;
};

__global__ void __launch_bounds__(768, 3)
gated_mix_kernel(const unsigned* __restrict__ x32, const void* __restrict__ kc, const void* __restrict__ vc,
                 const void* __restrict__ wq, const void* __restrict__ wo, const void* __restrict__ w1,
                 const void* __restrict__ w3, const void* __restrict__ w2, const unsigned* __restrict__ gw32,
                 __hip_bfloat16* __restrict__ out, unsigned char* ws) {
  __shared__ __attribute__((aligned(16))) Lds L;
  const int tid = threadIdx.x, lane = tid & 63;
  const int wave = __builtin_amdgcn_readfirstlane(tid >> 6);
  const int bid = blockIdx.x;
  const int s = (bid >> 3) & 7, h = (bid & 7) + 8 * (bid >> 6);
  const int r = lane >> 4, c = lane & 15;
  const bool mlp = h < NFG;
  const __amdgpu_buffer_rsrc_t rw = __builtin_amdgcn_make_buffer_rsrc(ws, 0, WS_END, RSRC_W3);
  ST(0);
  if (tid == 0) L.cnt = 0;
  wg_barrier();

  if (wave < 8) {
    const size_t slab = ((size_t)(s * NH + h) * SK + (SK - WIN)) * DH * 2;
    const __amdgpu_buffer_rsrc_t rk = __builtin_amdgcn_make_buffer_rsrc((char*)kc + slab, 0, WIN * DH * 2, RSRC_W3);
    const __amdgpu_buffer_rsrc_t rv = __builtin_amdgcn_make_buffer_rsrc((char*)vc + slab, 0, WIN * DH * 2, RSRC_W3);
    const int voff = tid * 16;
    u32x4 buf[64];
    auto issue = [&](auto J) {
      constexpr int j = J.v;
      if constexpr (j < 32) buf[j] = __builtin_amdgcn_raw_buffer_load_b128(rk, voff, j * 8192, KV_AUX);
      else if constexpr (j < 64) buf[j] = __builtin_amdgcn_raw_buffer_load_b128(rv, voff, (j - 32) * 8192, KV_AUX);
    };
    sfor<0, PRE>([&](auto J) {
      if constexpr (J.v >= FLY) wait_vm<FLY - 1>();
      issue(J);
      __builtin_amdgcn_sched_barrier(0);
    });
    ST(1);
    wg_barrier();
    ST(2);
    const u32x4 q = *(const u32x4*)&L.q[4 * c];
    const bool b3 = c & 8, b2 = c & 4, b1 = c & 2, b0 = c & 1;
    float t1[2], t2[2], t3[2], t4[2], sc[2];
    constexpr int NX = 32 + FLY - PRE;
    sfor<0, 32>([&](auto J) {
      constexpr int j = J.v, u = j & 1, i = j >> 1;
      const u32x4 k = buf[j];
      float v = dot2(k[3], q[3], dot2(k[2], q[2], dot2(k[1], q[1], dot2(k[0], q[0], 0.f))));
      if constexpr (!(i & 1)) t1[u] = v;
      else {
        v = fold<XOR8>(t1[u], v, b3);
        if constexpr (!(i & 2)) t2[u] = v;
        else {
          v = fold<XOR7>(t2[u], v, b2);
          if constexpr (!(i & 4)) t3[u] = v;
          else {
            v = fold<XOR2>(t3[u], v, b1);
            if constexpr (!(i & 8)) t4[u] = v; else sc[u] = fold<XOR1>(t4[u], v, b0);
          }
        }
      }
      constexpr int n0 = (j * NX + 23) / 24 < NX ? (j * NX + 23) / 24 : NX, n1 = ((j + 1) * NX + 23) / 24 < NX ? ((j + 1) * NX + 23) / 24 : NX;
      if constexpr (n1 > n0) {
        __builtin_amdgcn_sched_barrier(0);
        sfor<PRE + n0, PRE + n1>(issue);
        __builtin_amdgcn_sched_barrier(0);
      }
    });
    const float s0 = rbf(rbf(sc[0]) * 0.08838834764831845f), s1 = rbf(rbf(sc[1]) * 0.08838834764831845f);
    const float m = wave_max(fmaxf(s0, s1));
    const float p0 = __expf(s0 - m), p1 = __expf(s1 - m);
    const float l = wave_sum(p0 + p1);
    const unsigned pp = pk_bf16(p0, p1);
    if (lane == 0) *(f32x2*)&L.ml[wave][0] = f32x2{m, l};
    ST(3);
    float acc[8] = {0, 0, 0, 0, 0, 0, 0, 0};
    sfor<0, 16>([&](auto I) {
      constexpr int i = I.v, j = 32 + 2 * i;
      constexpr int src = ((i & 1) << 3) | ((i & 2) << 1) | ((i & 4) >> 1) | ((i & 8) >> 3);
      if constexpr (j + FLY < 64) { wait_vm<FLY - 1>(); issue(IC<j + FLY>{}); }
      if constexpr (j + 1 + FLY < 64) { __builtin_amdgcn_sched_barrier(0); wait_vm<FLY - 1>(); issue(IC<j + 1 + FLY>{}); }
      __builtin_amdgcn_sched_barrier(0);
      const u32x4 va = buf[j], vb = buf[j + 1];
      const unsigned pb = dppu<BCAST + src>(pp);
#pragma unroll
      for (int e = 0; e < 4; ++e) {
        acc[2 * e] = dot2(lo_pair(va[e], vb[e]), pb, acc[2 * e]);
        acc[2 * e + 1] = dot2(hi_pair(va[e], vb[e]), pb, acc[2 * e + 1]);
      }
      __builtin_amdgcn_sched_barrier(0);
    });
    *(f32x2*)&L.opart[wave][8 * c + 4 * (r >> 1) + 2 * (r & 1)] = rows_reduce8(acc);
    const unsigned tag = L.tag;
    ST(4);
    wg_barrier();
    if (wave == 0) {
      const f32x2 mw = *(const f32x2*)&L.ml[(lane + (lane >> 5) * 4) & 7][0];
      const int t = lane & 31, w0 = (lane >> 5) * 4;
      f32x4 part[4];
#pragma unroll
      for (int w = 0; w < 4; ++w) part[w] = *(const f32x4*)&L.opart[w0 + w][4 * t];
      float M = fmaxf(mw[0], dpp<XOR1>(mw[0])); M = fmaxf(M, dpp<XOR2>(M)); M = fmaxf(M, dpp<XOR7>(M));
      float f = __expf(mw[0] - M), ls = f * mw[1];
      ls += dpp<XOR1>(ls); ls += dpp<XOR2>(ls); ls += dpp<XOR7>(ls);
      f = f / ls;
      f32x4 o = part[0] * dpp<BCAST + 0>(f) + part[1] * dpp<BCAST + 1>(f) + part[2] * dpp<BCAST + 2>(f) + part[3] * dpp<BCAST + 3>(f);
      const float o0 = swap32_add(o[0], o[0]), o1 = swap32_add(o[1], o[1]), o2 = swap32_add(o[2], o[2]), o3 = swap32_add(o[3], o[3]);
      if (lane < 32)
        __builtin_amdgcn_raw_buffer_store_b128(u32x4{pk_bf16(o0, o1), tag, pk_bf16(o2, o3), tag}, rw,
                                               (((t >> 3) * 2 + (t & 1)) * 32 + s * 4 + ((t >> 1) & 3)) * 16, WS_X1 + h * 6144, SC1);
      ST(5);
    } else if (wave == 1 && h < 20) {
      u32x4 v[8];
      _Pragma("nounroll") for (unsigned spins = 0; spins < SPIN_MAX; ++spins) {
        bool ok = true;
#pragma unroll
        for (int i = 0; i < 8; ++i) v[i] = __builtin_amdgcn_raw_buffer_load_b128(rw, lane * 16 + (i & 3) * 1024, WS_X2 + (s * 20 + h) * 8192 + (i >> 2) * 4096, SC1);
#pragma unroll
        for (int i = 0; i < 8; ++i) ok &= v[i][1] == tag && v[i][3] == tag;
        if (__all(ok)) break;
        asm volatile("" ::: "memory");
      }
      float a0 = 0.f, a1 = 0.f;
#pragma unroll
      for (int i = 0; i < 8; ++i) { a0 += __uint_as_float(v[i][0]); a1 += __uint_as_float(v[i][2]); }
      a0 = swap32_add(a0, a1);
      a0 = swap16_add(a0, a0);
      if (!(r & 1)) out[((h & 3) * 2 + (r >> 1)) * DM + 80 * s + 16 * (h >> 2) + c] = __float2bfloat16(a0);
      if (bid == 0 && lane == 0) {
        gu64* ep = (gu64*)ws;
        __hip_atomic_store(ep, __hip_atomic_load((gu64*)(ws + WS_FLAG), RLX_AGENT), RLX_AGENT);
      }
      ST(6);
    }
  } else {
    __builtin_amdgcn_s_setprio(3);
    const int hw = wave - 8, ht = tid - 512;
    const unsigned long long epoch = __hip_atomic_load((gu64*)ws, RLX_AGENT);
    const bool act = (hw < 2 || mlp) && !(EXP & 4);
    u32x2 wa[20], xa[5];
    {
      const char* base = hw < 2 ? (const char*)wq + h * 256 + hw * 128 : (const char*)(hw == 2 ? w1 : w3) + h * 128;
      const int pitch = hw < 2 ? NH * DH * 2 : FF * 2;
      const __amdgpu_buffer_rsrc_t ra = __builtin_amdgcn_make_buffer_rsrc((char*)base + (size_t)80 * s * pitch, 0, act ? 79 * pitch + 128 : 0, RSRC_W3);
      const int voff = 20 * r * pitch + c * 8;
#pragma unroll
      for (int k = 0; k < 5; ++k) xa[k] = *(const u32x2*)(x32 + (c & 7) * (DM / 2) + 40 * s + 10 * r + 2 * k);
#pragma unroll
      for (int k = 0; k < 20; ++k) wa[k] = __builtin_amdgcn_raw_buffer_load_b64(ra, voff, k * pitch, W_AUX);
    }
    unsigned gx[2][5], gv[5];
#pragma unroll
    for (int k = 0; k < 5; ++k) { gv[k] = gw32[lane + 64 * k]; gx[0][k] = x32[2 * hw * (DM / 2) + lane + 64 * k]; gx[1][k] = x32[(2 * hw + 1) * (DM / 2) + lane + 64 * k]; }
    __builtin_amdgcn_sched_barrier(0);
    const unsigned long long tag64 = epoch * 0x9E3779B97F4A7C15ull + 0x632BE59BD9B4E019ull;
    const unsigned tag = (unsigned)(tag64 >> 32);
    if (!(EXP & 1)) {
      const u32x4 inv = {0u, ~tag, 0u, ~tag};
      const int bq = 2 * (r & 1) + (r >> 1);
      __builtin_amdgcn_raw_buffer_store_b128(inv, rw, bq * 8192 + h * 256 + c * 16, WS_X2 + (s * 20 + hw * 4) * 8192, SC1);
      if (hw == 0) __builtin_amdgcn_raw_buffer_store_b128(inv, rw, bq * 8192 + h * 256 + c * 16, WS_X2 + (s * 20 + 16) * 8192, SC1);
      if (hw == 1 && lane < 48) __builtin_amdgcn_raw_buffer_store_b128(inv, rw, ((lane >> 2) * 32 + s * 4 + (lane & 3)) * 16, WS_X1 + h * 6144, SC1);
    }
    {
      f32x4 d[4];
#pragma unroll
      for (int e = 0; e < 4; ++e) {
        unsigned bf[10];
#pragma unroll
        for (int k = 0; k < 10; ++k) bf[k] = (e & 1) ? hi_pair(wa[2 * k][e >> 1], wa[2 * k + 1][e >> 1]) : lo_pair(wa[2 * k][e >> 1], wa[2 * k + 1][e >> 1]);
        d[e] = mfma32(u32x4{xa[0][0], xa[0][1], xa[1][0], xa[1][1]}, u32x4{bf[0], bf[1], bf[2], bf[3]}, f32x4{0, 0, 0, 0});
        d[e] = mfma32(u32x4{xa[2][0], xa[2][1], xa[3][0], xa[3][1]}, u32x4{bf[4], bf[5], bf[6], bf[7]}, d[e]);
        d[e] = mfma16(xa[4], u32x2{bf[8], bf[9]}, d[e]);
      }
      const bool up = r >= 2;
#pragma unroll
      for (int i = 0; i < ((EXP & 8) ? (tag == 0x12345u ? 2 : 0) : 2); ++i) {
        const f32x4 v = {up ? d[0][i + 2] : d[0][i], up ? d[1][i + 2] : d[1][i], up ? d[2][i + 2] : d[2][i], up ? d[3][i + 2] : d[3][i]};
        __builtin_amdgcn_raw_buffer_store_b128(__builtin_bit_cast(u32x4, v), rw, (4 * (r & 1) + 2 * (r >> 1) + i) * 8192 + c * 16,
                                               WS_X0 + h * 65536 + s * 1024 + hw * 256, SC1);
      }
    }
    ST(1);
    wait_vm<0>();
    if (lane == 0 && __hip_atomic_fetch_add(&L.cnt, 1u, __ATOMIC_RELAXED, __HIP_MEMORY_SCOPE_WORKGROUP) == 3u)
      __hip_atomic_store((gu64*)(ws + WS_FLAG) + h * 8 + s, tag64, RLX_AGENT);
    ST(2);
    if (hw == 0) {
      _Pragma("nounroll") for (unsigned spins = 0; spins < SPIN_MAX; ++spins) {
        if (__all(__hip_atomic_load((gu64*)(ws + WS_FLAG) + h * 8 + (lane & 7), RLX_AGENT) == tag64)) break;
        __builtin_amdgcn_s_sleep(4);
      }
      asm volatile("" ::: "memory");
      ST(7);
      f32x4 a = {0, 0, 0, 0};
#pragma unroll
      for (int i = 0; i < ((EXP & 8) ? 1 : 8); ++i)
        a += __builtin_bit_cast(f32x4, __builtin_amdgcn_raw_buffer_load_b128(rw, lane * 16 + (i & 3) * 1024, WS_X0 + h * 65536 + s * 8192 + (i >> 2) * 4096, SC1));
      if (lane < 32) *(u32x2*)&L.q[2 * lane] = u32x2{pk_bf16(a[0], a[1]), pk_bf16(a[2], a[3])};
      if (lane == 0) L.tag = tag;
      float hv[4];
#pragma unroll
      for (int e = 0; e < 4; ++e) {
        const float ga = rbf(a[e]), up = __shfl(ga, lane | 16);
        hv[e] = rbf(ga / (1.f + __expf(-ga))) * up;
      }
      if (mlp && r == 2)
        __builtin_amdgcn_raw_buffer_store_b128(u32x4{pk_bf16(hv[0], hv[1]), tag, pk_bf16(hv[2], hv[3]), tag}, rw,
                                               ((8 + (c >> 3) * 2 + (c & 1)) * 32 + s * 4 + ((c >> 1) & 3)) * 16, WS_X1 + h * 6144, SC1);
    } else {
      const char* src = hw < 3 ? (const char*)wo + (h * 128 + (hw - 1) * 64) * 1280 : (const char*)w2 + (mlp ? h * 64 * 1280 : 0);
      const __amdgpu_buffer_rsrc_t ro = __builtin_amdgcn_make_buffer_rsrc((char*)src + s * 160, 0, (hw < 3 || mlp) && !(EXP & 2) ? 63 * 1280 + 160 : 0, RSRC_W3);
      u32x4 t[10];
#pragma unroll
      for (int k = 0; k < 10; ++k) {
        const int ch = lane + 64 * k, row = ch / 10, cc = ch % 10;
        t[k] = __builtin_amdgcn_raw_buffer_load_b128(ro, row * 1280 + cc * 16, 0, W_AUX);
      }
#pragma unroll
      for (int k = 0; k < 10; ++k) {
        const int ch = lane + 64 * k, row = ch / 10, cc = ch % 10;
        *(u32x4*)&L.wblk[(hw - 1) * 64 + row][cc * 8] = t[k];
      }
    }
    {
#pragma unroll
      for (int u = 0; u < 2; ++u) {
        float a = 0.f;
#pragma unroll
        for (int k = 0; k < 5; ++k) {
          a += rbf(__uint_as_float(gx[u][k] << 16) * __uint_as_float(gv[k] << 16));
          a += rbf(__uint_as_float(gx[u][k] & 0xffff0000u) * __uint_as_float(gv[k] & 0xffff0000u));
        }
        a = rbf(wave_sum(a));
        const float g = rbf(1.f / (1.f + __expf(-a)));
        if (lane == 0) *(f32x2*)&L.g[2 * hw + u][0] = f32x2{g, rbf(1.f - g)};
      }
    }
    ST(3);
    wg_barrier();
    u32x4 bfr[2][6];
#pragma unroll
    for (int tt = 0; tt < 2; ++tt)
      if (tt == 0 || hw == 0) {
        const int col = (tt ? 64 : 16 * hw) + c;
#pragma unroll
        for (int ks = 0; ks < 6; ++ks)
#pragma unroll
          for (int j = 0; j < 4; ++j)
            bfr[tt][ks][j] = (unsigned)L.wblk[32 * ks + 8 * r + 2 * j][col] | ((unsigned)L.wblk[32 * ks + 8 * r + 2 * j + 1][col] << 16);
      }
    const int bq = 2 * (r & 1) + (r >> 1);
    const f32x4 gg = *(const f32x4*)&L.g[2 * bq][0];
    const int aoff = ((c & 7) * 4 + r) * 16;
    f32x4 dm[2] = {{0, 0, 0, 0}, {0, 0, 0, 0}};
    if (mlp) {
      u32x4 v[4];
      _Pragma("nounroll") for (unsigned spins = 0; spins < SPIN_MAX; ++spins) {
        bool ok = true;
#pragma unroll
        for (int i = 0; i < 4; ++i) v[i] = __builtin_amdgcn_raw_buffer_load_b128(rw, aoff + i * 512, WS_X1 + h * 6144 + 4096, SC1);
#pragma unroll
        for (int i = 0; i < 4; ++i) ok &= v[i][1] == tag && v[i][3] == tag;
        if (__all(ok)) break;
        __builtin_amdgcn_s_sleep(8);
        asm volatile("" ::: "memory");
      }
#pragma unroll
      for (int ks = 0; ks < 2; ++ks) {
        const u32x4 a = {v[2 * ks][0], v[2 * ks][2], v[2 * ks + 1][0], v[2 * ks + 1][2]};
        dm[0] = mfma32(a, bfr[0][4 + ks], dm[0]);
        if (hw == 0) dm[1] = mfma32(a, bfr[1][4 + ks], dm[1]);
      }
    }
    if (hw == 3)
      _Pragma("nounroll") for (unsigned spins = 0; spins < SPIN_MAX; ++spins) {
        if (__all(__hip_atomic_load((gu64*)(ws + WS_FLAG) + (lane & 31) * 8 + s, RLX_AGENT) == tag64)) break;
        __builtin_amdgcn_s_sleep(8);
      }
    const bool up = r >= 2;
    float mz[2][2];
#pragma unroll
    for (int tt = 0; tt < 2; ++tt) { mz[tt][0] = gg[1] * (up ? dm[tt][2] : dm[tt][0]); mz[tt][1] = gg[3] * (up ? dm[tt][3] : dm[tt][1]); }
    ST(4);
    wg_barrier();
    u32x4 v[8];
    _Pragma("nounroll") for (unsigned spins = 0; spins < SPIN_MAX; ++spins) {
      bool ok = true;
#pragma unroll
      for (int i = 0; i < 8; ++i) v[i] = __builtin_amdgcn_raw_buffer_load_b128(rw, aoff + i * 512, WS_X1 + h * 6144, SC1);
#pragma unroll
      for (int i = 0; i < 8; ++i) ok &= v[i][1] == tag && v[i][3] == tag;
      if (__all(ok)) break;
      asm volatile("" ::: "memory");
    }
    ST(5);
#pragma unroll
    for (int tt = 0; tt < 2; ++tt)
      if (tt == 0 || hw == 0) {
        f32x4 d = {0, 0, 0, 0};
#pragma unroll
        for (int ks = 0; ks < 4; ++ks) d = mfma32(u32x4{v[2 * ks][0], v[2 * ks][2], v[2 * ks + 1][0], v[2 * ks + 1][2]}, bfr[tt][ks], d);
        const float y0 = gg[0] * (up ? d[2] : d[0]) + mz[tt][0], y1 = gg[2] * (up ? d[3] : d[1]) + mz[tt][1];
        __builtin_amdgcn_raw_buffer_store_b128(u32x4{__float_as_uint(y0), tag, __float_as_uint(y1), tag}, rw, bq * 8192 + h * 256 + c * 16,
                                               WS_X2 + (s * 20 + (tt ? 16 : hw * 4)) * 8192, SC1);
      }
    ST(6);
  }
  ST_FLUSH();
}

extern "C" void kernel_launch(void* const* d_in, const int* in_sizes, int n_in, void* d_out, int out_size,
                              void* d_ws, size_t ws_size, hipStream_t stream) {
  gated_mix_kernel<<<NB * NH, 768, 0, stream>>>((const unsigned*)d_in[0], d_in[1], d_in[2], d_in[3], d_in[4], d_in[6],
                                                d_in[7], d_in[8], (const unsigned*)d_in[9], (__hip_bfloat16*)d_out,
                                                (unsigned char*)d_ws);
}
```

```cpp
#include <hip/hip_runtime.h>
#include <stdint.h>

constexpr int PRE = 28;
#define FLY 6
constexpr int TRIG = 18;
#define TEND 1980
#define TB 360
#define TI 50
constexpr int KV_AUX = 2, W_AUX = 2;

typedef unsigned u32x4 __attribute__((ext_vector_type(4)));
typedef float f32x4 __attribute__((ext_vector_type(4)));
typedef float f32x2 __attribute__((ext_vector_type(2)));
typedef __bf16 bf16x2 __attribute__((ext_vector_type(2)));
typedef __attribute__((address_space(1))) unsigned long long gu64;
#define RLX_AGENT __ATOMIC_RELAXED, __HIP_MEMORY_SCOPE_AGENT

constexpr int NB = 8, DM = 640, NH = 32, SK = 4096, DH = 128, WIN = 1024, FF = 1728;
constexpr int NFG = FF / 64;
constexpr int RSRC_W3 = 0x00020000;
constexpr int SC1 = 16;
constexpr unsigned WS_X0 = 4096;
constexpr unsigned WS_X1 = WS_X0 + NH * 8192;
constexpr unsigned WS_X2 = WS_X1 + NH * 4096;
constexpr unsigned WS_END = WS_X2 + 80 * 4096;
constexpr unsigned SPIN_MAX = 1u << 20;
typedef unsigned u32x2 __attribute__((ext_vector_type(2)));
typedef __bf16 bf16x8 __attribute__((ext_vector_type(8)));
typedef short s16x4 __attribute__((ext_vector_type(4)));

__device__ __forceinline__ float dot2(unsigned a, unsigned b, float c) {
  return __builtin_amdgcn_fdot2_f32_bf16(__builtin_bit_cast(bf16x2, a), __builtin_bit_cast(bf16x2, b), c, false);
}
__device__ __forceinline__ unsigned pk_bf16(float lo, float hi) {
  f32x2 v = {lo, hi};
  return __builtin_bit_cast(unsigned, __builtin_convertvector(v, bf16x2));
}
__device__ __forceinline__ float rbf(float x) { return __uint_as_float(pk_bf16(x, x) << 16); }
__device__ __forceinline__ unsigned lo_pair(unsigned a, unsigned b) { return __builtin_amdgcn_perm(b, a, 0x05040100u); }
__device__ __forceinline__ unsigned hi_pair(unsigned a, unsigned b) { return __builtin_amdgcn_perm(b, a, 0x07060302u); }
template <int CTRL> __device__ __forceinline__ float dpp(float x) {
  return __builtin_bit_cast(float, __builtin_amdgcn_mov_dpp(__builtin_bit_cast(int, x), CTRL, 0xf, 0xf, true));
}
template <int CTRL> __device__ __forceinline__ unsigned dppu(unsigned x) {
  return (unsigned)__builtin_amdgcn_mov_dpp((int)x, CTRL, 0xf, 0xf, true);
}
constexpr int XOR1 = 0xB1, XOR2 = 0x4E, XOR7 = 0x141, XOR8 = 0x128, BCAST = 0x150;
__device__ __forceinline__ float swap32_add(float a, float b) {
  auto t = __builtin_amdgcn_permlane32_swap(__float_as_uint(a), __float_as_uint(b), false, false);
  return __uint_as_float(t[0]) + __uint_as_float(t[1]);
}
__device__ __forceinline__ float swap16_add(float a, float b) {
  auto t = __builtin_amdgcn_permlane16_swap(__float_as_uint(a), __float_as_uint(b), false, false);
  return __uint_as_float(t[0]) + __uint_as_float(t[1]);
}
__device__ __forceinline__ float wave_max(float x) {
  x = fmaxf(x, dpp<XOR1>(x)); x = fmaxf(x, dpp<XOR2>(x)); x = fmaxf(x, dpp<XOR7>(x)); x = fmaxf(x, dpp<XOR8>(x));
  auto s = __builtin_amdgcn_permlane16_swap(__float_as_uint(x), __float_as_uint(x), false, false);
  x = fmaxf(__uint_as_float(s[0]), __uint_as_float(s[1]));
  auto t = __builtin_amdgcn_permlane32_swap(__float_as_uint(x), __float_as_uint(x), false, false);
  return fmaxf(__uint_as_float(t[0]), __uint_as_float(t[1]));
}
__device__ __forceinline__ float wave_sum(float x) {
  x += dpp<XOR1>(x); x += dpp<XOR2>(x); x += dpp<XOR7>(x); x += dpp<XOR8>(x);
  x = swap16_add(x, x);
  return swap32_add(x, x);
}
template <int CTRL> __device__ __forceinline__ float fold(float lo, float hi, bool bit) {
  return (bit ? hi : lo) + dpp<CTRL>(bit ? lo : hi);
}
__device__ __forceinline__ f32x4 mfma32(u32x4 a, u32x4 b, f32x4 c) {
  return __builtin_amdgcn_mfma_f32_16x16x32_bf16(__builtin_bit_cast(bf16x8, a), __builtin_bit_cast(bf16x8, b), c, 0, 0, 0);
}
__device__ __forceinline__ f32x4 mfma16(u32x2 a, u32x2 b, f32x4 c) {
  return __builtin_amdgcn_mfma_f32_16x16x16bf16_1k(__builtin_bit_cast(s16x4, a), __builtin_bit_cast(s16x4, b), c, 0, 0, 0);
}
template <int N> __device__ __forceinline__ void wait_vm() { asm volatile("s_waitcnt vmcnt(%0)" ::"n"(N) : "memory"); }
__device__ __forceinline__ f32x2 rows_reduce8(const float (&a)[8]) {
  float n0 = swap32_add(a[0], a[4]), n1 = swap32_add(a[1], a[5]), n2 = swap32_add(a[2], a[6]), n3 = swap32_add(a[3], a[7]);
  f32x2 r = {swap16_add(n0, n2), swap16_add(n1, n3)};
  return r;
}
__device__ __forceinline__ void wg_barrier() {
  asm volatile("s_waitcnt lgkmcnt(0)\n\ts_barrier" ::: "memory");
}
template <int N> struct IC { static constexpr int v = N; };
template <int I, int E, class F> __device__ __forceinline__ void sfor(F&& f) { if constexpr (I < E) { f(IC<I>{}); sfor<I + 1, E>(f); } }

struct Nop { __device__ void operator()(bool) const {} };
template <int N, int STEP, int NAP, class F = Nop>
__device__ __forceinline__ void poll(u32x2 (&v)[N], __amdgpu_buffer_rsrc_t rw, int voff, unsigned tl, unsigned th, F each = F()) {
  int a[N];
#pragma unroll
  for (int i = 0; i < N; ++i) { a[i] = voff; v[i] = u32x2{0u, 0u}; }
  auto round = [&]() {
    u32x4 t[N];
#pragma unroll
    for (int i = 0; i < N; ++i) t[i] = __builtin_amdgcn_raw_buffer_load_b128(rw, a[i] + (i * STEP & 4095), i * STEP & ~4095, SC1);
    int all = -1;
#pragma unroll
    for (int i = 0; i < N; ++i) {
      const bool hit = a[i] >= 0 && t[i][1] == tl && t[i][3] == th;
      v[i] = hit ? u32x2{t[i][0], t[i][2]} : v[i];
      a[i] = hit ? (int)0x80000000 : a[i];
      all &= a[i];
    }
    asm volatile("" ::: "memory");
    return __all(all < 0);
  };
  _Pragma("nounroll") for (unsigned spins = 0; spins < SPIN_MAX; ++spins) {
    const bool done = round();
    each(done);
    if (done) break;
    if (NAP) __builtin_amdgcn_s_sleep(NAP);
  }
}
template <int NAP> __device__ __forceinline__ void lds_wait(unsigned* p, unsigned n) {
  _Pragma("nounroll") for (unsigned spins = 0; spins < 64 * SPIN_MAX; ++spins) {
    if (__hip_atomic_load(p, __ATOMIC_ACQUIRE, __HIP_MEMORY_SCOPE_WORKGROUP) >= n) break;
    __builtin_amdgcn_s_sleep(NAP);
  }
}
__device__ __forceinline__ void lds_post(unsigned* p, int lane) {
  asm volatile("s_waitcnt lgkmcnt(0)" ::: "memory");
  if (lane == 0) __hip_atomic_fetch_add(p, 1u, __ATOMIC_RELAXED, __HIP_MEMORY_SCOPE_WORKGROUP);
}

struct Lds {
  f32x4 part[8][2][64], msum[2][64];
  float opart[8][128];
  float ml[8][2];
  unsigned q[64];
  float g[8][2];
  unsigned qcnt, cnt, trig[2];
};

__global__ void __launch_bounds__(768, 3)
gated_mix_kernel(const unsigned* x32, const void* kc, const void* vc,
                 const void* wq, const void* wo, const void* w1,
                 const void* w3, const void* w2, const unsigned* __restrict__ gw32,
                 unsigned* out32, unsigned char* ws) {
  __shared__ __attribute__((aligned(16))) Lds L;
  const int tid = threadIdx.x, lane = tid & 63;
  const int wave = __builtin_amdgcn_readfirstlane(tid >> 6);
  const int bid = blockIdx.x;
  const int s = (bid >> 3) & 7, h = 4 * (bid & 7) + (bid >> 6);
  const int r = lane >> 4, c = lane & 15;
  const __amdgpu_buffer_rsrc_t rw = __builtin_amdgcn_make_buffer_rsrc(ws, 0, WS_END, RSRC_W3);
  auto get_tag = [&]() {
    unsigned long long p = (unsigned long long)ws;
    asm volatile("" : "+s"(p));
    return *(const __attribute__((address_space(4))) unsigned long long*)p * 0x9E3779B97F4A7C15ull + 0x632BE59BD9B4E019ull;
  };

  if (wave >= 4) {
    const size_t slab = ((size_t)(s * NH + h) * SK + (SK - WIN)) * DH * 2;
    const __amdgpu_buffer_rsrc_t rk = __builtin_amdgcn_make_buffer_rsrc((char*)kc + slab, 0, WIN * DH * 2, RSRC_W3);
    const __amdgpu_buffer_rsrc_t rv = __builtin_amdgcn_make_buffer_rsrc((char*)vc + slab, 0, WIN * DH * 2, RSRC_W3);
    const int sw = wave - 4, voff = (tid - 256) * 16;
    const bool busy = bid < 26 * 8 && !(bid >= 14 * 8 && bid < 16 * 8 && (bid & 7) >= NFG - 24);
    const unsigned slope = busy ? (TEND - TB) * 64 / 61 : (TEND - TI) * 64 / 61;
    const unsigned t0 = ((unsigned)__builtin_amdgcn_s_memrealtime() + (busy ? TB : TI)) * 64u;
    if (tid == 256) { L.qcnt = 0; L.cnt = 0; L.trig[0] = 0; L.trig[1] = 0; }
    wg_barrier();
    u32x4 buf[64];
    auto issue = [&](auto J) {
      constexpr int j = J.v;
      if constexpr (j >= 6 && j < 64 && j % 2 == 0) {
        _Pragma("nounroll") while ((int)((unsigned)__builtin_amdgcn_s_memrealtime() * 64u - t0 - (j - 3) * slope) < 0) __builtin_amdgcn_s_sleep(1);
      }
      if constexpr (j < 32) buf[j] = __builtin_amdgcn_raw_buffer_load_b128(rk, voff, j * 8192, KV_AUX);
      else if constexpr (j < 64) buf[j] = __builtin_amdgcn_raw_buffer_load_b128(rv, voff, (j - 32) * 8192, KV_AUX);
    };
    sfor<0, PRE>([&](auto J) {
      if constexpr (J.v >= FLY) wait_vm<FLY - 1>();
      issue(J);
      if constexpr (J.v == TRIG) __hip_atomic_store(&L.trig[0], 1u, __ATOMIC_RELAXED, __HIP_MEMORY_SCOPE_WORKGROUP);
      __builtin_amdgcn_sched_barrier(0);
    });
    unsigned long long tag64 = get_tag();
    asm volatile("" : "+s"(tag64));
    lds_wait<1>(&L.qcnt, 1);
    const u32x4 q = *(const u32x4*)&L.q[4 * c];
    const bool b3 = c & 8, b2 = c & 4, b1 = c & 2, b0 = c & 1;
    float t1[2], t2[2], t3[2], t4[2], sc[2];
    constexpr int NX = 32 + FLY - PRE;
    sfor<0, 32>([&](auto J) {
      constexpr int j = J.v, u = j & 1, i = j >> 1;
      const u32x4 k = buf[j];
      float v = dot2(k[3], q[3], dot2(k[2], q[2], dot2(k[1], q[1], dot2(k[0], q[0], 0.f))));
      if constexpr (!(i & 1)) t1[u] = v;
      else {
        v = fold<XOR8>(t1[u], v, b3);
        if constexpr (!(i & 2)) t2[u] = v;
        else {
          v = fold<XOR7>(t2[u], v, b2);
          if constexpr (!(i & 4)) t3[u] = v;
          else {
            v = fold<XOR2>(t3[u], v, b1);
            if constexpr (!(i & 8)) t4[u] = v; else sc[u] = fold<XOR1>(t4[u], v, b0);
          }
        }
      }
      constexpr int n0 = (j * NX + 23) / 24 < NX ? (j * NX + 23) / 24 : NX, n1 = ((j + 1) * NX + 23) / 24 < NX ? ((j + 1) * NX + 23) / 24 : NX;
      if constexpr (n1 > n0) {
        __builtin_amdgcn_sched_barrier(0);
        sfor<PRE + n0, PRE + n1>(issue);
        __builtin_amdgcn_sched_barrier(0);
      }
    });
    const float s0 = rbf(rbf(sc[0]) * 0.08838834764831845f), s1 = rbf(rbf(sc[1]) * 0.08838834764831845f);
    const float m = wave_max(fmaxf(s0, s1));
    const float p0 = __expf(s0 - m), p1 = __expf(s1 - m);
    const float l = wave_sum(p0 + p1);
    const unsigned pp = pk_bf16(p0, p1);
    if (lane == 0) *(f32x2*)&L.ml[sw][0] = f32x2{m, l};
    __hip_atomic_store(&L.trig[1], 1u, __ATOMIC_RELAXED, __HIP_MEMORY_SCOPE_WORKGROUP);
    float acc[8] = {0, 0, 0, 0, 0, 0, 0, 0};
    sfor<0, 16>([&](auto I) {
      constexpr int i = I.v, j = 32 + 2 * i;
      constexpr int src = ((i & 1) << 3) | ((i & 2) << 1) | ((i & 4) >> 1) | ((i & 8) >> 3);
      if constexpr (j + FLY < 64) { wait_vm<FLY - 1>(); issue(IC<j + FLY>{}); }
      if constexpr (j + 1 + FLY < 64) { __builtin_amdgcn_sched_barrier(0); wait_vm<FLY - 1>(); issue(IC<j + 1 + FLY>{}); }
      __builtin_amdgcn_sched_barrier(0);
      const u32x4 va = buf[j], vb = buf[j + 1];
      const unsigned pb = dppu<BCAST + src>(pp);
#pragma unroll
      for (int e = 0; e < 4; ++e) {
        acc[2 * e] = dot2(lo_pair(va[e], vb[e]), pb, acc[2 * e]);
        acc[2 * e + 1] = dot2(hi_pair(va[e], vb[e]), pb, acc[2 * e + 1]);
      }
      __builtin_amdgcn_sched_barrier(0);
    });
    *(f32x2*)&L.opart[sw][8 * c + 4 * (r >> 1) + 2 * (r & 1)] = rows_reduce8(acc);
    wg_barrier();
    if (sw == 0) {
      const f32x2 mw = *(const f32x2*)&L.ml[(lane + (lane >> 5) * 4) & 7][0];
      const int t = lane & 31, w0 = (lane >> 5) * 4;
      f32x4 part[4];
#pragma unroll
      for (int w = 0; w < 4; ++w) part[w] = *(const f32x4*)&L.opart[w0 + w][4 * t];
      float M = fmaxf(mw[0], dpp<XOR1>(mw[0])); M = fmaxf(M, dpp<XOR2>(M)); M = fmaxf(M, dpp<XOR7>(M));
      float f = __expf(mw[0] - M), ls = f * mw[1];
      ls += dpp<XOR1>(ls); ls += dpp<XOR2>(ls); ls += dpp<XOR7>(ls);
      f = f / ls;
      f32x4 o = part[0] * dpp<BCAST + 0>(f) + part[1] * dpp<BCAST + 1>(f) + part[2] * dpp<BCAST + 2>(f) + part[3] * dpp<BCAST + 3>(f);
      const float o0 = swap32_add(o[0], o[0]), o1 = swap32_add(o[1], o[1]), o2 = swap32_add(o[2], o[2]), o3 = swap32_add(o[3], o[3]);
      if (lane < 32)
        __builtin_amdgcn_raw_buffer_store_b128(u32x4{pk_bf16(o0, o1), (unsigned)tag64, pk_bf16(o2, o3), (unsigned)(tag64 >> 32)}, rw,
                                               t * 16, WS_X1 + h * 4096 + s * 512, SC1);
    }
  } else {
    __builtin_amdgcn_s_setprio(3);
    const int hw = wave, c8 = c & 7, rg = lane >> 3, li = bid >> 3;
    unsigned long long tag64;
    unsigned tl, th;
    auto start = [&]() {
      wg_barrier();
        tag64 = get_tag();
      asm volatile("" : "+s"(tag64));
      tl = (unsigned)tag64; th = (unsigned)(tag64 >> 32);
    };
    auto pair = [](int e, u32x4 a, u32x4 b) { return (e & 1) ? hi_pair(a[e >> 1], b[e >> 1]) : lo_pair(a[e >> 1], b[e >> 1]); };
    auto put = [&](const f32x4 (&d)[8]) {
      if ((r < 2) == (c < 8)) {
#pragma unroll
        for (int i = 0; i < 8; ++i)
          L.part[2 * hw + (c >> 3)][i >> 2][(4 * (r & 1) + (i & 3)) * 8 + c8] = f32x4{d[i & 4][i & 3], d[(i & 4) + 1][i & 3], d[(i & 4) + 2][i & 3], d[(i & 4) + 3][i & 3]};
      }
    };
    auto reduce = [&](const f32x4 (&d)[8], unsigned* cnt, f32x4 (&sum)[2]) {
      put(d);
      asm volatile("s_waitcnt lgkmcnt(0)" ::: "memory");
      unsigned n = 0;
      if (lane == 0) n = __hip_atomic_fetch_add(cnt, 1u, __ATOMIC_RELAXED, __HIP_MEMORY_SCOPE_WORKGROUP);
      if (__builtin_amdgcn_readfirstlane(n) != 3u) return false;
      asm volatile("" ::: "memory");
#pragma unroll
      for (int i = 0; i < 2; ++i)
        sum[i] = ((L.part[0][i][lane] + L.part[1][i][lane]) + (L.part[2][i][lane] + L.part[3][i][lane])) +
                 ((L.part[4][i][lane] + L.part[5][i][lane]) + (L.part[6][i][lane] + L.part[7][i][lane]));
      return true;
    };
    auto fetch_q = [&]() {
      u32x2 v[1];
      lds_wait<4>(&L.trig[0], 1);
      poll<1, 0, 8>(v, rw, lane < 32 ? WS_X0 + h * 8192 + rg * 1024 + s * 128 + c8 * 16 : (int)0x80000000, tl, th);
      if (lane < 32) *(u32x2*)&L.q[32 * r + 4 * c8 + 2 * (c >> 3)] = v[0];
      lds_post(&L.qcnt, lane);
    };
    if (li < 16) {
      const int hm = (bid & 7) + 8 * (li >> 2), hp = 4 * (bid & 7) + (li >> 2), blk = li & 3;
      const bool act = blk < 2 || hm < NFG;
      const char* base = blk < 2 ? (const char*)wq + hp * 256 + blk * 128 : (const char*)(blk == 2 ? w1 : w3) + hm * 128;
      const int pitch = blk < 2 ? NH * DH * 2 : FF * 2;
      const __amdgpu_buffer_rsrc_t ra = __builtin_amdgcn_make_buffer_rsrc((char*)base + (size_t)160 * hw * pitch, 0, act ? 159 * pitch + 128 : 0, RSRC_W3);
      const int voff = 20 * rg * pitch + c8 * 16;
      u32x4 wa[20];
      u32x2 xa[5];
#pragma unroll
      for (int k = 0; k < 20; ++k) wa[k] = __builtin_amdgcn_raw_buffer_load_b128(ra, voff, k * pitch, W_AUX);
#pragma unroll
      for (int k = 0; k < 5; ++k) xa[k] = *(const u32x2*)(x32 + c8 * (DM / 2) + 80 * hw + 10 * rg + 2 * k);
      start();
      if (act) {
        f32x4 d[8], a[2];
#pragma unroll
        for (int e = 0; e < 8; ++e) {
          unsigned bf[10];
#pragma unroll
          for (int k = 0; k < 10; ++k) bf[k] = pair(e, wa[2 * k], wa[2 * k + 1]);
          d[e] = mfma32(u32x4{xa[0][0], xa[0][1], xa[1][0], xa[1][1]}, u32x4{bf[0], bf[1], bf[2], bf[3]}, f32x4{0, 0, 0, 0});
          d[e] = mfma32(u32x4{xa[2][0], xa[2][1], xa[3][0], xa[3][1]}, u32x4{bf[4], bf[5], bf[6], bf[7]}, d[e]);
          d[e] = mfma16(xa[4], u32x2{bf[8], bf[9]}, d[e]);
        }
        if (reduce(d, &L.cnt, a)) {
#pragma unroll
          for (int i = 0; i < 2; ++i)
            __builtin_amdgcn_raw_buffer_store_b128(u32x4{pk_bf16(a[i][0], a[i][1]), tl, pk_bf16(a[i][2], a[i][3]), th}, rw, lane * 16 + i * 1024, WS_X0 + hp * 8192 + blk * 2048, SC1);
        }
      }
      if (hw == 0) fetch_q();
      else if (hw == 1 && li < 10) {
        wg_barrier();
        u32x2 v[4];
        const __amdgpu_buffer_rsrc_t ry = __builtin_amdgcn_make_buffer_rsrc(out32, 0, NB * DM * 2, RSRC_W3);
        auto fin = [&](bool done) {
          float a0 = 0.f, a1 = 0.f;
#pragma unroll
          for (int i = 0; i < 4; ++i) { a0 += __uint_as_float(v[i][0]); a1 += __uint_as_float(v[i][1]); }
          int yo = done && lane < 32 ? ((bid & 7) * (DM / 2) + 32 * li + 4 * c8 + rg) * 4 : (int)0x80000000;
          asm volatile("" : "+v"(yo));
          __builtin_amdgcn_raw_buffer_store_b32(pk_bf16(swap32_add(a0, a0), swap32_add(a1, a1)), ry, yo, 0, 0);
        };
        poll<4, 1024, 1>(v, rw, WS_X2 + bid * 4096 + lane * 16, tl, th, fin);
        if (bid == 0 && lane == 0) __hip_atomic_store((gu64*)ws, tag64, RLX_AGENT);
      }
    } else if (li < 26) {
      const int cb = li - 16, hm = (bid & 7) + 8 * hw, hh = 4 * (bid & 7) + hw;
      const bool mlp = hm < NFG;
      const __amdgpu_buffer_rsrc_t ro = __builtin_amdgcn_make_buffer_rsrc((char*)wo + hh * 128 * 1280 + cb * 128, 0, 127 * 1280 + 128, RSRC_W3);
      const __amdgpu_buffer_rsrc_t r2 = __builtin_amdgcn_make_buffer_rsrc((char*)w2 + (mlp ? hm * 64 * 1280 + cb * 128 : 0), 0, mlp ? 63 * 1280 + 128 : 0, RSRC_W3);
      u32x4 wb[16], wc[8];
      unsigned gx[2][5], gv[5];
#pragma unroll
      for (int k = 0; k < 16; ++k) wb[k] = __builtin_amdgcn_raw_buffer_load_b128(ro, 4 * rg * 1280 + c8 * 16 + (k & 3) * 1280, (k >> 2) * 32 * 1280, W_AUX);
#pragma unroll
      for (int k = 0; k < 8; ++k) wc[k] = __builtin_amdgcn_raw_buffer_load_b128(r2, 8 * rg * 1280 + c8 * 16 + (k & 3) * 1280, (k >> 2) * 4 * 1280, W_AUX);
      start();
#pragma unroll
      for (int k = 0; k < 5; ++k) { gv[k] = gw32[lane + 64 * k]; gx[0][k] = x32[2 * hw * (DM / 2) + lane + 64 * k]; gx[1][k] = x32[(2 * hw + 1) * (DM / 2) + lane + 64 * k]; }
      u32x4 bfr[8][2];
#pragma unroll
      for (int ks = 0; ks < 2; ++ks)
#pragma unroll
        for (int e = 0; e < 8; ++e) {
          bfr[e][ks] = u32x4{pair(e, wb[8 * ks], wb[8 * ks + 1]), pair(e, wb[8 * ks + 2], wb[8 * ks + 3]), pair(e, wb[8 * ks + 4], wb[8 * ks + 5]), pair(e, wb[8 * ks + 6], wb[8 * ks + 7])};
          asm volatile("" : "+v"(bfr[e][ks]));
        }
#pragma unroll
      for (int u = 0; u < 2; ++u) {
        float a = 0.f;
#pragma unroll
        for (int k = 0; k < 5; ++k) {
          a += rbf(__uint_as_float(gx[u][k] << 16) * __uint_as_float(gv[k] << 16));
          a += rbf(__uint_as_float(gx[u][k] & 0xffff0000u) * __uint_as_float(gv[k] & 0xffff0000u));
        }
        a = rbf(wave_sum(a));
        const float g = rbf(1.f / (1.f + __expf(-a)));
        if (lane == 0) *(f32x2*)&L.g[2 * hw + u][0] = f32x2{g, rbf(1.f - g)};
      }
      if (hw == 0) fetch_q();
      u32x2 v[4];
      f32x4 d[8], y[2];
      u32x4 hf = {0u, 0u, 0u, 0u};
      if (mlp) {
        lds_wait<8>(&L.trig[1], 1);
        poll<4, 1024, 8>(v, rw, WS_X0 + hh * 8192 + 4096 + c8 * 128 + rg * 16, tl, th);
#pragma unroll
        for (int k = 0; k < 4; ++k) {
          const unsigned ga = v[k >> 1][k & 1], u = v[2 + (k >> 1)][k & 1];
          const float g0 = __uint_as_float(ga << 16), g1 = __uint_as_float(ga & 0xffff0000u);
          hf[k] = pk_bf16(rbf(g0 / (1.f + __expf(-g0))) * __uint_as_float(u << 16), rbf(g1 / (1.f + __expf(-g1))) * __uint_as_float(u & 0xffff0000u));
        }
      }
#pragma unroll
      for (int e = 0; e < 8; ++e)
        d[e] = mfma32(hf, u32x4{pair(e, wc[0], wc[1]), pair(e, wc[2], wc[3]), pair(e, wc[4], wc[5]), pair(e, wc[6], wc[7])}, f32x4{0, 0, 0, 0});
      if (reduce(d, &L.cnt, y)) { const float gb = L.g[rg][1]; L.msum[0][lane] = gb * y[0]; L.msum[1][lane] = gb * y[1]; }
      wg_barrier();
      const float ga = L.g[rg][0];
      const f32x2 m = ((const f32x2*)&L.msum[hw >> 1][lane])[hw & 1];
      poll<4, 128, 1>(v, rw, WS_X1 + hh * 4096 + c8 * 512 + rg * 16, tl, th);
#pragma unroll
      for (int e = 0; e < 8; ++e) d[e] = mfma32(u32x4{v[0][0], v[0][1], v[1][0], v[1][1]}, bfr[e][0], f32x4{0, 0, 0, 0});
#pragma unroll
      for (int e = 0; e < 8; ++e) d[e] = mfma32(u32x4{v[2][0], v[2][1], v[3][0], v[3][1]}, bfr[e][1], d[e]);
      put(d);
      wg_barrier();
      f32x2 z[8];
#pragma unroll
      for (int k = 0; k < 8; ++k) z[k] = ((const f32x2*)&L.part[k][hw >> 1][lane])[hw & 1];
      const f32x2 yy = ga * (((z[0] + z[1]) + (z[2] + z[3])) + ((z[4] + z[5]) + (z[6] + z[7]))) + m;
      __builtin_amdgcn_raw_buffer_store_b128(u32x4{__float_as_uint(yy[0]), tl, __float_as_uint(yy[1]), th}, rw, rg * 4096 + c8 * 16,
                                             WS_X2 + cb * 32768 + (bid & 7) * 512 + hw * 128, SC1);
    } else {
      start();
      if (hw == 0) fetch_q();
    }
  }
}

extern "C" void kernel_launch(void* const* d_in, const int* in_sizes, int n_in, void* d_out, int out_size,
                              void* d_ws, size_t ws_size, hipStream_t stream) {
  gated_mix_kernel<<<NB * NH, 768, 0, stream>>>((const unsigned*)d_in[0], d_in[1], d_in[2], d_in[3], d_in[4], d_in[6],
                                                d_in[7], d_in[8], (const unsigned*)d_in[9], (unsigned*)d_out,
                                                (unsigned char*)d_ws);
}
```
